```python
import torch

def _softmax(x, axis=-1):
    return torch.softmax(x, dim=axis)
def _gelu(x):
    return torch.nn.functional.gelu(x, approximate="tanh")
def _silu(x):
    return torch.nn.functional.silu(x)
def _sigmoid(x):
    return torch.sigmoid(x)
def _layernorm(x, axis=-1, eps=1e-5):
    m = x.mean(dim=axis, keepdim=True)
    v = x.var(dim=axis, keepdim=True, unbiased=False)
    return (x - m) / torch.sqrt(v + eps)
def _scatter_add(tgt, idx, src, axis=0):
    return tgt.clone().index_add_(axis, idx, src)
def _segment_sum(src, seg, n):
    out = torch.zeros((n,) + src.shape[1:], dtype=src.dtype)
    return out.index_add_(0, seg, src)
def _pad(x, pw):
    flat = tuple(v for pair in reversed(pw) for v in pair)
    return torch.nn.functional.pad(x, flat)

kernel_name = 'parallel_sum__ssm_conv_s5__dense_activation_relu__prompt'

def setup_inputs():

    _w = (lambda a: (lambda t: t.to(torch.bfloat16) if t.is_floating_point() else t)(torch.as_tensor(a)))
    def _A_setup():
        B, S, D = 1, 2048, 512
        P = D
        Lambda_re = -0.5 * (lambda _x: torch.exp(torch.as_tensor(_x)))((lambda *s: torch.randn(*s, dtype=torch.bfloat16) * 0.8)(P))
        Lambda_im = (lambda *s: torch.randn(*s, dtype=torch.bfloat16) * 0.8)(P) * 1.0
        B_re = (lambda *s: torch.randn(*s, dtype=torch.bfloat16) * 0.8)(P, D) * (D ** -0.5)
        B_im = (lambda *s: torch.randn(*s, dtype=torch.bfloat16) * 0.8)(P, D) * (D ** -0.5)
        C_re = (lambda *s: torch.randn(*s, dtype=torch.bfloat16) * 0.8)(D, P) * (P ** -0.5)
        C_im = (lambda *s: torch.randn(*s, dtype=torch.bfloat16) * 0.8)(D, P) * (P ** -0.5)
        log_dt = (lambda *s: torch.randn(*s, dtype=torch.bfloat16) * 0.8)(P) * 0.3 - 2.0
        Dskip = (lambda *s: torch.randn(*s, dtype=torch.bfloat16) * 0.8)(D)
        return {
            "x": _w((lambda *s: torch.randn(*s, dtype=torch.bfloat16) * 0.05)(B, S, D)),
            "Lambda_re": _w(Lambda_re),
            "Lambda_im": _w(Lambda_im),
            "B_re": _w(B_re),
            "B_im": _w(B_im),
            "C_re": _w(C_re),
            "C_im": _w(C_im),
            "log_dt": _w(log_dt),
            "Dskip": _w(Dskip),
        }
    _A_in = _A_setup()

    def _B_setup():
        B, S, D, Dff = 1, 2048, 512, 1376
        return {
            "x": _w((lambda *s: torch.randn(*s, dtype=torch.bfloat16) * 0.05)(B, S, D)),
            "w1": _w((lambda *s: torch.randn(*s, dtype=torch.bfloat16) * 0.05)(D, Dff)),
            "w2": _w((lambda *s: torch.randn(*s, dtype=torch.bfloat16) * 0.05)(Dff, D)),
        }
    _B_in = _B_setup()

    return {
        "A_x": _A_in["x"],
        "A_Lambda_re": _A_in["Lambda_re"],
        "A_Lambda_im": _A_in["Lambda_im"],
        "A_B_re": _A_in["B_re"],
        "A_B_im": _A_in["B_im"],
        "A_C_re": _A_in["C_re"],
        "A_C_im": _A_in["C_im"],
        "A_log_dt": _A_in["log_dt"],
        "A_Dskip": _A_in["Dskip"],
        "B_x": _B_in["x"],
        "B_w1": _B_in["w1"],
        "B_w2": _B_in["w2"],
    }

def reference(A_x, A_Lambda_re, A_Lambda_im, A_B_re, A_B_im, A_C_re, A_C_im, A_log_dt, A_Dskip, B_x, B_w1, B_w2):
    def _A_ref(x, Lambda_re, Lambda_im, B_re, B_im, C_re, C_im, log_dt, Dskip):
        Bsz, L, H = x.shape
        Lam = Lambda_re + 1j * Lambda_im
        Bmat = B_re + 1j * B_im
        Cmat = C_re + 1j * C_im
        dt = torch.exp(log_dt)
        Lam_bar = torch.exp(dt * Lam)
        B_bar = ((Lam_bar - 1.0) / Lam)[:, None] * Bmat
        Bu = torch.einsum("bld,pd->blp", x + 0j, B_bar)
        h = torch.zeros_like(Bu[:, 0, :])
        hs = []
        for t in range(L):
            h = Lam_bar[None, :] * h + Bu[:, t, :]
            hs.append(h)
        hseq = torch.stack(hs, 1)
        y = 2.0 * torch.einsum("blp,dp->bld", hseq, Cmat).real + x * Dskip[None, None, :]
        return y

    def _B_ref(x, w1, w2):
        h = torch.einsum("bsd,df->bsf", x, w1)
        zero = torch.zeros_like(h)
        h = torch.maximum(h, zero)
        return torch.einsum("bsf,fd->bsd", h, w2)

    x = A_x
    xn = _layernorm(x, axis=-1)
    return x + _A_ref(xn, A_Lambda_re, A_Lambda_im, A_B_re, A_B_im, A_C_re, A_C_im, A_log_dt, A_Dskip) + _B_ref(xn, B_w1, B_w2)
```

```cpp
#include <hip/hip_runtime.h>
#include <hip/hip_bf16.h>
#include <stdint.h>

#define DI __device__ __forceinline__
#define LAS __attribute__((address_space(3)))
typedef __attribute__((ext_vector_type(8))) __bf16 bf16x8;
typedef __attribute__((ext_vector_type(2))) __bf16 bf16x2;
typedef __attribute__((ext_vector_type(2))) float f32x2;
typedef __attribute__((ext_vector_type(4))) float f32x4;
typedef __attribute__((ext_vector_type(16))) float f32x16;
typedef __attribute__((ext_vector_type(2))) unsigned u32x2;
typedef __attribute__((ext_vector_type(4))) unsigned u32x4;
typedef __attribute__((ext_vector_type(4))) short s16x4;
typedef unsigned long long u64;
typedef __amdgpu_buffer_rsrc_t rsrc_t;

constexpr int L_ = 2048, D_ = 512, F_ = 1376;
constexpr int KSF = F_ / 16;
constexpr int KSS = 64;
constexpr int NSF = 22;
constexpr int RNG = 9;
constexpr int DEP = 8;

constexpr unsigned WS_EPOCH = 0, WS_S = 4096, WS_FLAGF = 0x30000, WS_FLAGS = 0x40000;
constexpr unsigned WS_H1 = 0x100000, WS_H = 0x700000, WS_END = 0xB00000;
constexpr unsigned WS_STAMP = 0xC00000;

constexpr unsigned SLOT = 36864, OFF_B = 16384, OFF_W = 24576;
constexpr unsigned MISC = 4 * SLOT;
constexpr unsigned M_STATS = MISC, M_CS = MISC + 1024, M_EW = MISC + 1536, M_TR = MISC + 4096;
constexpr unsigned P2_FE = 0, P2_SG0 = 36 * 1024, P2_SG1 = 72 * 1024, P2_FL = 104 * 1024;
constexpr unsigned LDS_BYTES = 163840;
constexpr int SPIN_CAP = 1 << 18;

DI rsrc_t mkrsrc(const void* p, unsigned bytes) { return __builtin_amdgcn_make_buffer_rsrc((void*)p, (short)0, (int)bytes, 0x00020000); }
DI void dma16(unsigned lds, unsigned voff, rsrc_t srd, unsigned soff) {
  asm volatile("s_mov_b32 m0, %0\n\ts_nop 0\n\tbuffer_load_dwordx4 %1, %2, %3 offen lds" ::"s"(lds), "v"(voff), "s"(srd), "s"(soff) : "m0", "memory");
}
DI void ldu16(unsigned& dst, unsigned voff, rsrc_t srd) {
  asm volatile("buffer_load_ushort %0, %1, %2, 0 offen" : "=v"(dst) : "v"(voff), "s"(srd) : "memory");
}
DI void ldepoch(u32x2& dst, unsigned voff, rsrc_t srd) {
  asm volatile("buffer_load_dwordx2 %0, %1, %2, 0 offen sc1" : "=v"(dst) : "v"(voff), "s"(srd) : "memory");
}
DI unsigned opaque(unsigned v) { asm volatile("" : "+v"(v)); return v; }
DI void sched0() { __builtin_amdgcn_sched_barrier(0); }
DI void ldact(u32x4& dst, unsigned voff, rsrc_t srd, unsigned soff) {
  asm volatile("buffer_load_dwordx4 %0, %1, %2, %3 offen sc1" : "=v"(dst) : "v"(voff), "s"(srd), "s"(soff) : "memory");
}
template <int N> DI void vmwait() { asm volatile("s_waitcnt vmcnt(%0)" ::"n"(N) : "memory"); }
template <int N> DI void vmwait2(u32x4& a, u32x4& b) { asm volatile("s_waitcnt vmcnt(%2)" : "+v"(a), "+v"(b) : "n"(N) : "memory"); }
DI void lgkm0() { asm volatile("s_waitcnt lgkmcnt(0)" ::: "memory"); }
DI void wg_barrier() { asm volatile("s_waitcnt lgkmcnt(0)\n\ts_barrier" ::: "memory"); }

DI float dot2(unsigned a, unsigned b, float c) {
  return __builtin_amdgcn_fdot2_f32_bf16(__builtin_bit_cast(bf16x2, a), __builtin_bit_cast(bf16x2, b), c, false);
}
DI f32x16 mfma(u32x4 a, u32x4 b, f32x16 c) {
  return __builtin_amdgcn_mfma_f32_32x32x16_bf16(__builtin_bit_cast(bf16x8, a), __builtin_bit_cast(bf16x8, b), c, 0, 0, 0);
}
DI float solo(float v) { asm("" : "+v"(v)); return v; }
DI unsigned pack2(float lo, float hi) {
  f32x2 v = {solo(lo), solo(hi)};
  return __builtin_bit_cast(unsigned, __builtin_convertvector(v, bf16x2));
}
DI float bf16r(float v) { return __uint_as_float(pack2(v, 0.f) << 16); }
DI float bflo(unsigned u) { return __uint_as_float(u << 16); }
DI float bfhi(unsigned u) { return __uint_as_float(u & 0xffff0000u); }
DI float lo32(float v) { auto r = __builtin_amdgcn_permlane32_swap(__float_as_uint(v), __float_as_uint(v), false, false); return __uint_as_float(r[0]); }
DI float hi32(float v) { auto r = __builtin_amdgcn_permlane32_swap(__float_as_uint(v), __float_as_uint(v), false, false); return __uint_as_float(r[1]); }
DI float sum32(float v) { auto r = __builtin_amdgcn_permlane32_swap(__float_as_uint(v), __float_as_uint(v), false, false); return solo(__uint_as_float(r[0]) + __uint_as_float(r[1])); }

struct cpx { float r, i; };
DI cpx cmul(cpx a, cpx b) { return {fmaf(a.r, b.r, -solo(a.i * b.i)), fmaf(a.r, b.i, solo(a.i * b.r))}; }
DI cpx cfma(cpx a, cpx b, cpx c) { return {fmaf(a.r, b.r, solo(fmaf(-a.i, b.i, c.r))), fmaf(a.r, b.i, solo(fmaf(a.i, b.r, c.i)))}; }

typedef __attribute__((ext_vector_type(4))) short v4i16_t;
DI u32x2 trread(LAS char* p) { return __builtin_bit_cast(u32x2, __builtin_amdgcn_ds_read_tr16_b64_v4i16((LAS v4i16_t*)p)); }
DI u32x4 ld128(LAS char* p) { return *(LAS u32x4*)p; }
DI f32x4 ldf4(LAS char* p) { return *(LAS f32x4*)p; }

constexpr int vmF(int i) {
  int pos = 0, pa[NSF] = {}, pw[NSF + 1] = {};
  for (int k = 0; k < DEP; ++k) { pos += 2; pa[k] = pos; }
  pos += 2; pw[RNG] = pos;
  for (int it = 0; it < i; ++it) {
    if (it + 1 + RNG < NSF) { pos += 2; pw[it + 1 + RNG] = pos; }
    if (it + DEP < NSF) { pos += 2; pa[it + DEP] = pos; }
  }
  int need = pa[i] > pw[i + 1] ? pa[i] : pw[i + 1];
  return pos - need;
}
constexpr int vmS(int u) {
  int pos = 0, pa[16] = {}, pg[5] = {};
  for (int k = 0; k < DEP; ++k) { pos += 2; pa[k] = pos; }
  for (int it = 0; it < u; ++it) {
    if (it == 2) { pos += 8; pg[2] = pos; }
    if (it == 6) { pos += 8; pg[3] = pos; }
    if (it + DEP < 16) { pos += 2; pa[it + DEP] = pos; }
  }
  int need = pa[u] > pg[(u + 1) >> 2] ? pa[u] : pg[(u + 1) >> 2];
  return pos - need;
}

struct Params {
  const void *x, *lre, *lim, *bre, *bim, *cre, *cim, *ldt, *dsk, *w1, *w2;
  float* out;
  char* ws;
};

#define STAMP(k) do {} while (0)

template <int I, class T> DI void f_steps(T&& fn) { if constexpr (I < NSF) { fn(std::integral_constant<int, I>{}); f_steps<I + 1>(fn); } }
template <int I, class T> DI void s_steps(T&& fn) { if constexpr (I < 16) { fn(std::integral_constant<int, I>{}); s_steps<I + 1>(fn); } }
template <int I, class T> DI void stages(T&& fn) { if constexpr (I < 8) { fn(std::integral_constant<int, I>{}); stages<I + 1>(fn); } }

DI void write_partial(LAS char* smem, unsigned base0, unsigned base1, const f32x16 (&acc)[2][2], int lane) {
  const int c = lane & 31, h = lane >> 5;
#pragma unroll
  for (int tt = 0; tt < 2; ++tt)
#pragma unroll
    for (int dt = 0; dt < 2; ++dt)
#pragma unroll
      for (int i = 0; i < 4; ++i) {
        f32x4 v = {acc[dt][tt][4 * i], acc[dt][tt][4 * i + 1], acc[dt][tt][4 * i + 2], acc[dt][tt][4 * i + 3]};
        const int slot = 8 * dt + 2 * i + h;
        *(LAS f32x4*)(smem + (tt ? base1 : base0) + c * 256 + ((slot ^ (c & 15)) << 4)) = v;
      }
}

__global__ void __launch_bounds__(512, 2) s5ffn_kernel(Params p) {
  __shared__ __attribute__((aligned(16))) char smem_[LDS_BYTES];
  LAS char* smem = (LAS char*)smem_;
  const unsigned lds0 = (unsigned)(uintptr_t)smem;
  const int tid = threadIdx.x, lane = tid & 63;
  const int w = __builtin_amdgcn_readfirstlane(tid >> 6);
  const int g = w & 3;
  const int xcd = blockIdx.x & 7, r = blockIdx.x >> 3;
  const int panel = 4 * (xcd >> 1) + (r >> 3), slice = 8 * (xcd & 1) + (r & 7);
  const int rh = (r >> 2) & 1, cb = 4 * (xcd & 1) + (r & 3);
  const int l31 = lane & 31, h = lane >> 5;
  const bool isS = w < 4;
  STAMP(0);

  const rsrc_t rX = mkrsrc(p.x, L_ * D_ * 2), rW1 = mkrsrc(p.w1, D_ * F_ * 2);
  const rsrc_t rB = mkrsrc(isS ? p.bre : p.bim, 512 * D_ * 2);
  const rsrc_t rWS = mkrsrc(p.ws, WS_END);

  const unsigned vX0 = (16 * w + (lane >> 3)) * 1024 + (((lane & 7) ^ (lane >> 4)) << 4);
  const unsigned vX1 = (16 * w + 8 + (lane >> 3)) * 1024 + (((lane & 7) ^ (4 + (lane >> 4))) << 4);
  const unsigned vB = (32 * slice + 8 * g + (lane >> 3)) * 1024 + (((lane & 7) ^ ((4 * w + (lane >> 4)) & 7)) << 4);
  auto w1off = [&](int wi) -> unsigned {
    const int c = 64 * wi + lane, row = c / 12, cc = c - 12 * row;
    const int colb = min(192 * slice + 16 * cc, F_ * 2 - 16);
    return row * (F_ * 2) + colb;
  };
  const int wiA = isS ? 2 * g : 8 + g;
  const unsigned vWa = w1off(wiA), vWb = w1off(2 * g + 1);
  const unsigned sXbase = panel * 131072;

  auto issue_stage = [&](int st) {
    const unsigned sl = lds0 + (st & 3) * SLOT;
    dma16(sl + (2 * w) * 1024, vX0, rX, sXbase + st * 128);
    dma16(sl + (2 * w + 1) * 1024, vX1, rX, sXbase + st * 128);
    dma16(sl + OFF_B + w * 1024, vB, rB, st * 128);
    dma16(sl + OFF_W + wiA * 1024, vWa, rW1, st * 64 * F_ * 2);
    if (isS) dma16(sl + OFF_W + (2 * g + 1) * 1024, vWb, rW1, st * 64 * F_ * 2);
  };

  asm volatile("s_nop 4" ::: "memory");
  u32x2 epoch;
  unsigned plre = 0, plim = 0, pldt = 0;
  ldepoch(epoch, WS_EPOCH, rWS);
  if (isS) {
    ldu16(plre, slice * 64 + l31 * 2, mkrsrc(p.lre, 1024));
    ldu16(plim, slice * 64 + l31 * 2, mkrsrc(p.lim, 1024));
    ldu16(pldt, slice * 64 + l31 * 2, mkrsrc(p.ldt, 1024));
  }
  issue_stage(0); issue_stage(1); issue_stage(2); issue_stage(3);
  if (isS) asm volatile("s_waitcnt vmcnt(20)" : "+v"(epoch), "+v"(plre), "+v"(plim), "+v"(pldt)::"memory");
  else asm volatile("s_waitcnt vmcnt(16)" : "+v"(epoch)::"memory");
  const u64 tag = (((u64)epoch[1] << 32) | epoch[0]) + 1;

  if (isS) {
    const rsrc_t rCre = mkrsrc(p.cre, D_ * 512 * 2), rCim = mkrsrc(p.cim, D_ * 512 * 2);
    const int q = g;
    const unsigned vC0 = (64 * cb + (lane >> 3)) * 1024;
    auto issue_group = [&](int grp, unsigned ldsb) {
      const rsrc_t rc = (grp & 1) ? rCim : rCre;
      const unsigned so = (128 * q + 64 * (grp >> 1)) * 2;
#pragma unroll
      for (int k = 0; k < 8; ++k) {
        const unsigned vo = vC0 + k * 8192 + (((lane & 7) ^ ((4 * k + (lane >> 4)) & 7)) << 4);
        dma16(lds0 + ldsb + k * 1024, vo, rc, so);
      }
    };
    const int pr = 16 * ((l31 >> 2) & 1) + 4 * (l31 >> 3) + (l31 & 3);
    const int rowx = 32 * g + pr;
    const float lr = bflo(plre), li = bflo(plim), dt = bf16r(expf(bflo(pldt)));
    cpx lam, c2;
    {
      const float er = expf(dt * lr);
      float sn, cs;
      sincosf(dt * li, &sn, &cs);
      lam = {solo(er * cs), solo(er * sn)};
      const float nr = lam.r - 1.f, ni = lam.i, id = 2.f / fmaf(lr, lr, solo(li * li));
      c2 = {solo(fmaf(nr, lr, solo(ni * li)) * id), solo(fmaf(ni, lr, -solo(nr * li)) * id)};
    }
    cpx l16 = cmul(lam, lam); l16 = cmul(l16, l16); l16 = cmul(l16, l16); l16 = cmul(l16, l16);
    const cpx l32 = cmul(l16, l16);
    cpx l128 = cmul(l32, l32); l128 = cmul(l128, l128);
    const unsigned aX0 = rowx * 128 + ((h ^ ((rowx >> 1) & 7)) << 4);
    const unsigned aB0 = OFF_B + l31 * 128 + ((h ^ ((l31 >> 1) & 7)) << 4);
    f32x16 are = {}, aim = {};
    float sum0 = 0.f, sum1 = 0.f, sq0 = 0.f, sq1 = 0.f, csr = 0.f, csi = 0.f;
    const unsigned ONES = 0x3F803F80u;
    u32x4 xf, br, bi;
    unsigned ax, ab_;
    auto rdS = [&](int sub) { xf = ld128(smem + (ax ^ (sub << 5))); br = ld128(smem + (ab_ ^ (sub << 5))); bi = ld128(smem + (ab_ ^ (sub << 5)) + 4096); };
    vmwait<15>();
    wg_barrier();
    ax = opaque(aX0); ab_ = opaque(aB0);
    rdS(0);
    stages<0>([&](auto ST) {
      constexpr int st = ST.value;
#pragma unroll
      for (int sub = 0; sub < 4; ++sub) {
        const u32x4 cx = xf, cr = br, ci = bi;
        if (sub < 3) rdS(sub + 1);
        else if (st < 7) {
          lgkm0();
          vmwait<st <= 4 ? 10 : st == 5 ? 5 : 8>();
          wg_barrier();
          if constexpr (st <= 3) issue_stage(st + 4);
          if constexpr (st == 5) issue_group(0, P2_SG0 + q * 8192);
          if constexpr (st == 6) issue_group(1, P2_SG1 + q * 8192);
          ax = opaque(aX0 + ((st + 1) & 3) * SLOT); ab_ = opaque(aB0 + ((st + 1) & 3) * SLOT);
          rdS(0);
        }
        are = mfma(cx, cr, are);
        aim = mfma(cx, ci, aim);
#pragma unroll
        for (int k = 0; k < 4; k += 2) {
          sum0 = dot2(cx[k], ONES, sum0); sum1 = dot2(cx[k + 1], ONES, sum1);
          sq0 = dot2(cx[k], cx[k], sq0); sq1 = dot2(cx[k + 1], cx[k + 1], sq1);
          csr = dot2(cr[k], ONES, csr); csr = dot2(cr[k + 1], ONES, csr);
          csi = dot2(ci[k], ONES, csi); csi = dot2(ci[k + 1], ONES, csi);
        }
        asm volatile("" : "+v"(sum0), "+v"(sum1), "+v"(sq0), "+v"(sq1), "+v"(csr), "+v"(csi));
        sched0();
      }
    });
    STAMP(1);
    {
      const float mean = solo(sum32(sum0 + sum1) * (1.f / 512.f));
      const float var = fmaxf(sum32(sq0 + sq1) * (1.f / 512.f) - mean * mean, 0.f);
      const float m = bf16r(mean);
      const float den = bf16r(__fsqrt_rn(bf16r(bf16r(var) + 1e-5f)));
      const float a = 1.f / den;
      if (h == 0) *(LAS f32x2*)(smem + M_STATS + rowx * 8) = f32x2{solo(a), solo(-m * a)};
    }
    csr = sum32(csr); csi = sum32(csi);
    wg_barrier();
    float ur[16], ui[16];
#pragma unroll
    for (int i = 0; i < 16; i += 2) {
      const f32x4 ab = ldf4(smem + M_STATS + (32 * g + 16 * h + i) * 8);
      ur[i] = fmaf(ab[0], are[i], ab[1] * csr); ui[i] = fmaf(ab[0], aim[i], ab[1] * csi);
      ur[i + 1] = fmaf(ab[2], are[i + 1], ab[3] * csr); ui[i + 1] = fmaf(ab[2], aim[i + 1], ab[3] * csi);
    }
    cpx e = {ur[0], ui[0]};
#pragma unroll
    for (int i = 1; i < 16; ++i) e = cfma(lam, e, cpx{ur[i], ui[i]});
    const cpx e0 = {lo32(e.r), lo32(e.i)}, e1 = {hi32(e.r), hi32(e.i)};
    const cpx ew = cfma(l16, e0, e1);
    if (h == 0) *(LAS f32x2*)(smem + M_EW + (g * 32 + l31) * 8) = f32x2{solo(ew.r), solo(ew.i)};
    wg_barrier();
    cpx pre = {0.f, 0.f};
    {
      cpx s = {0.f, 0.f};
#pragma unroll
      for (int k = 0; k < 4; ++k) {
        if (k == g) pre = s;
        const f32x2 v = *(LAS f32x2*)(smem + M_EW + (k * 32 + l31) * 8);
        s = cfma(l32, s, cpx{v[0], v[1]});
      }
      if (g == 3 && h == 0) {
        u32x4 gr = {__float_as_uint(s.r), (unsigned)tag, __float_as_uint(s.i), (unsigned)(tag >> 32)};
        __builtin_amdgcn_raw_buffer_store_b128(gr, rWS, WS_S + ((panel * 16 + slice) * 32 + l31) * 16, 0, 16);
      }
    }
    STAMP(2);
    cpx hin = {0.f, 0.f};
    if (panel > 0) {
      u32x4 gv[8];
      for (int spin = 0; spin < SPIN_CAP; ++spin) {
        bool ok = true;
#pragma unroll
        for (int k = 0; k < 8; ++k) {
          const int pp = min(8 * h + k, panel - 1);
          gv[k] = __builtin_amdgcn_raw_buffer_load_b128(rWS, WS_S + ((pp * 16 + slice) * 32 + l31) * 16, 0, 16);
        }
#pragma unroll
        for (int k = 0; k < 8; ++k) ok = ok && gv[k][1] == (unsigned)tag && gv[k][3] == (unsigned)(tag >> 32);
        if (__all(ok)) break;
        __builtin_amdgcn_s_sleep(1);
      }
      cpx acc = {0.f, 0.f}, pw = {1.f, 0.f};
#pragma unroll
      for (int k = 0; k < 8; ++k) {
        const bool v = 8 * h + k < panel;
        const cpx na = cfma(l128, acc, cpx{__uint_as_float(gv[k][0]), __uint_as_float(gv[k][2])});
        const cpx np = cmul(l128, pw);
        acc.r = v ? na.r : acc.r; acc.i = v ? na.i : acc.i;
        pw.r = v ? np.r : pw.r; pw.i = v ? np.i : pw.i;
      }
      hin = cfma(cpx{hi32(pw.r), hi32(pw.i)}, cpx{lo32(acc.r), lo32(acc.i)}, cpx{hi32(acc.r), hi32(acc.i)});
      hin = {solo(hin.r), solo(hin.i)};
    }
    STAMP(3);
    {
      cpx lg = {1.f, 0.f};
#pragma unroll
      for (int k = 0; k < 3; ++k) { const cpx t = cmul(lg, l32); if (k < g) lg = t; }
      cpx c = cfma(lg, hin, pre);
      const cpx c1 = cfma(l16, c, e0);
      if (h) c = c1;
      unsigned ore[8], oim[8];
#pragma unroll
      for (int i = 0; i < 16; i += 2) {
        c = cfma(lam, c, cpx{ur[i], ui[i]});
        const float r0 = fmaf(c2.r, c.r, -solo(c2.i * c.i)), i0 = fmaf(-c2.r, c.i, -solo(c2.i * c.r));
        c = cfma(lam, c, cpx{ur[i + 1], ui[i + 1]});
        const float r1 = fmaf(c2.r, c.r, -solo(c2.i * c.i)), i1 = fmaf(-c2.r, c.i, -solo(c2.i * c.r));
        ore[i >> 1] = pack2(r0, r1); oim[i >> 1] = pack2(i0, i1);
      }
      LAS char* tr = smem + M_TR + g * 2048;
      const int g16 = lane >> 4, q4 = (lane & 15) >> 2, p4 = lane & 3;
      const unsigned trb = (8 * (g16 >> 1) + q4) * 64 + (16 * (g16 & 1) + 4 * p4) * 2;
      const unsigned tg = 4 * panel + g;
#pragma unroll
      for (int part = 0; part < 2; ++part) {
        const unsigned* o = part ? oim : ore;
        *(LAS u32x4*)(tr + l31 * 64 + 32 * h) = u32x4{o[0], o[1], o[2], o[3]};
        *(LAS u32x4*)(tr + l31 * 64 + 32 * h + 16) = u32x4{o[4], o[5], o[6], o[7]};
        lgkm0();
#pragma unroll
        for (int s = 0; s < 2; ++s) {
          const u32x2 a = trread(tr + trb + s * 1024), b = trread(tr + trb + s * 1024 + 256);
          __builtin_amdgcn_raw_buffer_store_b128(u32x4{a[0], a[1], b[0], b[1]}, rWS,
                                                 WS_H + (tg * KSS + 4 * slice + 2 * part + s) * 1024 + lane * 16, 0, 16);
        }
        lgkm0();
      }
      vmwait<0>();
      if (lane == 0)
        __hip_atomic_store((u64*)(p.ws + WS_FLAGS + ((panel * 4 + g) * 16 + slice) * 64), tag, __ATOMIC_RELAXED, __HIP_MEMORY_SCOPE_AGENT);
    }
    STAMP(4);
    {
      const u64* fl = (const u64*)(p.ws + WS_FLAGS + ((panel * 4 + 2 * rh + ((lane >> 2) & 1)) * 16 + 4 * q + (lane & 3)) * 64);
      for (int spin = 0; spin < SPIN_CAP; ++spin) {
        const u64 v = __hip_atomic_load(fl, __ATOMIC_RELAXED, __HIP_MEMORY_SCOPE_AGENT);
        if (__all(v == tag)) break;
        __builtin_amdgcn_s_sleep(1);
      }
    }
    STAMP(5);
    f32x16 acc[2][2] = {};
    u32x4 act[DEP][2];
    const unsigned vL = lane * 16;
    const unsigned hb = WS_H + (4 * panel + 2 * rh) * KSS * 1024;
    auto ksS = [&](int u) { return 4 * (4 * q + 2 * (u >> 3) + ((u & 3) >> 1)) + 2 * ((u >> 2) & 1) + (u & 1); };
    auto issue_actS = [&](int u) {
      ldact(act[u % DEP][0], vL, rWS, hb + ksS(u) * 1024);
      ldact(act[u % DEP][1], vL, rWS, hb + (KSS + ksS(u)) * 1024);
    };
#pragma unroll
    for (int u = 0; u < DEP; ++u) issue_actS(u);
    const unsigned aC0 = l31 * 128 + ((h ^ ((l31 >> 1) & 7)) << 4) + q * 8192;
    const unsigned aCa = opaque(aC0 + P2_SG0), aCb = opaque(aC0 + P2_SG1);
    u32x4 c0, c1;
    auto rdC = [&](int u) { const unsigned a = (((u >> 2) & 1) ? aCb : aCa) ^ ((u & 3) << 5); c0 = ld128(smem + a); c1 = ld128(smem + a + 4096); };
    rdC(0);
    s_steps<0>([&](auto U) {
      constexpr int u = U.value;
      vmwait2<vmS(u)>(act[u % DEP][0], act[u % DEP][1]);
      const u32x4 w0 = c0, w1 = c1;
      if constexpr (u < 15) rdC(u + 1);
      acc[0][0] = mfma(w0, act[u % DEP][0], acc[0][0]);
      acc[0][1] = mfma(w0, act[u % DEP][1], acc[0][1]);
      acc[1][0] = mfma(w1, act[u % DEP][0], acc[1][0]);
      acc[1][1] = mfma(w1, act[u % DEP][1], acc[1][1]);
      if constexpr (u == 2 || u == 6) { lgkm0(); issue_group(u == 2 ? 2 : 3, (u == 2 ? P2_SG0 : P2_SG1) + q * 8192); }
      if constexpr (u + DEP < 16) issue_actS(u + DEP);
      sched0();
    });
    STAMP(6);
    write_partial(smem, P2_SG0 + q * 8192, P2_SG1 + q * 8192, acc, lane);
  } else {
    const rsrc_t rW2 = mkrsrc(p.w2, F_ * D_ * 2);
    const int q = g;
    const int nst = min(NSF, KSF - NSF * q);
    const unsigned vW2 = (lane >> 3) * 1024 + 128 * cb + (((lane & 7) ^ (((lane >> 4) & 1) << 2)) << 4);
    auto ring = [&](int i) -> unsigned { return i < 4 ? P2_FE + q * 8192 + i * 2048 : P2_FL + q * 10240 + (i - 4) * 2048; };
    auto issue_w2 = [&](int s) {
      const int ks = min(NSF * q + s, KSF - 1);
      const unsigned lb = lds0 + ring(s % RNG);
      dma16(lb, vW2, rW2, ks * 16 * 1024);
      dma16(lb + 1024, vW2, rW2, ks * 16 * 1024 + 8192);
    };
    const int rowx = 32 * g + l31;
    const int g16 = lane >> 4, q4 = (lane & 15) >> 2, p4 = lane & 3;
    const unsigned aX0 = rowx * 128 + ((h ^ ((rowx >> 1) & 7)) << 4);
    const unsigned aW0 = OFF_W + (8 * (g16 >> 1) + q4) * 192 + (16 * (g16 & 1) + 4 * p4) * 2;
    f32x16 a1[3] = {};
    float cs[3] = {0.f, 0.f, 0.f};
    const unsigned ONES = 0x3F803F80u;
    u32x4 xf, wf[3];
    unsigned ax, aw;
    auto rdF = [&](int sub) {
      xf = ld128(smem + (ax ^ (sub << 5)));
#pragma unroll
      for (int t = 0; t < 3; ++t) {
        const u32x2 a = trread(smem + aw + sub * 16 * 192 + t * 64), b = trread(smem + aw + (sub * 16 + 4) * 192 + t * 64);
        wf[t] = u32x4{a[0], a[1], b[0], b[1]};
      }
    };
    vmwait<12>();
    wg_barrier();
    ax = opaque(aX0); aw = opaque(aW0);
    rdF(0);
    stages<0>([&](auto ST) {
      constexpr int st = ST.value;
#pragma unroll
      for (int sub = 0; sub < 4; ++sub) {
        const u32x4 cx = xf, c0 = wf[0], c1 = wf[1], c2 = wf[2];
        if (sub < 3) rdF(sub + 1);
        else if (st < 7) {
          lgkm0();
          vmwait<st <= 4 ? 8 : st == 5 ? 12 : 8>();
          wg_barrier();
          if constexpr (st <= 3) issue_stage(st + 4);
          if constexpr (st == 4) { issue_w2(0); issue_w2(1); issue_w2(2); issue_w2(3); }
          ax = opaque(aX0 + ((st + 1) & 3) * SLOT); aw = opaque(aW0 + ((st + 1) & 3) * SLOT);
          rdF(0);
        }
        a1[0] = mfma(c0, cx, a1[0]);
        a1[1] = mfma(c1, cx, a1[1]);
        a1[2] = mfma(c2, cx, a1[2]);
#pragma unroll
        for (int k = 0; k < 4; ++k) { cs[0] = dot2(c0[k], ONES, cs[0]); cs[1] = dot2(c1[k], ONES, cs[1]); cs[2] = dot2(c2[k], ONES, cs[2]); }
        asm volatile("" : "+v"(cs[0]), "+v"(cs[1]), "+v"(cs[2]));
        sched0();
      }
    });
    STAMP(1);
#pragma unroll
    for (int t = 0; t < 3; ++t) {
      const float v = sum32(cs[t]);
      if (g == 0 && h == 0) *(LAS float*)(smem + M_CS + (32 * t + l31) * 4) = v;
    }
    wg_barrier();
    {
      const f32x2 ab = *(LAS f32x2*)(smem + M_STATS + rowx * 8);
      const unsigned tg = 4 * panel + g;
#pragma unroll
      for (int t = 0; t < 3; ++t) {
        unsigned o[8];
#pragma unroll
        for (int i = 0; i < 4; ++i) {
          const f32x4 c4 = ldf4(smem + M_CS + (32 * t + 8 * i + 4 * h) * 4);
          float v[4];
#pragma unroll
          for (int k = 0; k < 4; ++k) v[k] = fmaxf(fmaf(ab[0], a1[t][4 * i + k], ab[1] * c4[k]), 0.f);
          o[2 * i] = pack2(v[0], v[1]); o[2 * i + 1] = pack2(v[2], v[3]);
        }
#pragma unroll
        for (int s = 0; s < 2; ++s) {
          const int ks = 6 * slice + 2 * t + s;
          if (ks < KSF)
            __builtin_amdgcn_raw_buffer_store_b128(u32x4{o[4 * s], o[4 * s + 1], o[4 * s + 2], o[4 * s + 3]}, rWS,
                                                   WS_H1 + (tg * KSF + ks) * 1024 + lane * 16, 0, 16);
        }
      }
    }
    __builtin_amdgcn_sched_barrier(0);
    asm volatile("s_barrier" ::: "memory");
#pragma unroll
    for (int s = 4; s < RNG; ++s) issue_w2(s);
    vmwait<2 * (RNG - 4)>();
    if (lane == 0)
      __hip_atomic_store((u64*)(p.ws + WS_FLAGF + ((panel * 4 + g) * 16 + slice) * 64), tag, __ATOMIC_RELAXED, __HIP_MEMORY_SCOPE_AGENT);
    STAMP(2);
    {
      const int slo = (NSF * q) / 6, shi = (NSF * q + nst - 1) / 6;
      const int sl = min(slo + (lane & 7), shi);
      const u64* fl = (const u64*)(p.ws + WS_FLAGF + ((panel * 4 + 2 * rh + ((lane >> 3) & 1)) * 16 + sl) * 64);
      for (int spin = 0; spin < SPIN_CAP; ++spin) {
        const u64 v = __hip_atomic_load(fl, __ATOMIC_RELAXED, __HIP_MEMORY_SCOPE_AGENT);
        if (__all(v == tag)) break;
        __builtin_amdgcn_s_sleep(1);
      }
    }
    vmwait<0>();
    STAMP(5);
    f32x16 acc[2][2] = {};
    u32x4 act[DEP][2];
    const unsigned vL = lane * 16;
    const unsigned hb = WS_H1 + (4 * panel + 2 * rh) * KSF * 1024;
    auto issue_actF = [&](int s) {
      const int ks = min(NSF * q + s, KSF - 1);
      ldact(act[s % DEP][0], vL, rWS, hb + ks * 1024);
      ldact(act[s % DEP][1], vL, rWS, hb + (KSF + ks) * 1024);
    };
#pragma unroll
    for (int s = 0; s < DEP; ++s) issue_actF(s);
    const unsigned aT = (4 * (g16 >> 1) + q4) * 128;
    const unsigned colb = (16 * (g16 & 1) + 4 * p4) * 2, swp = ((q4 >> 1) & 1) << 6;
    const unsigned aT0 = aT + (colb ^ swp), aT1 = aT + ((64 + colb) ^ swp);
    u32x4 n0, n1;
    auto rdW = [&](int s) {
      const unsigned sb = ring(s % RNG);
      const unsigned t0 = opaque(aT0 + sb), t1 = opaque(aT1 + sb);
      const u32x2 a0 = trread(smem + t0), b0 = trread(smem + t0 + 1024), a1_ = trread(smem + t1), b1 = trread(smem + t1 + 1024);
      n0 = u32x4{a0[0], a0[1], b0[0], b0[1]}; n1 = u32x4{a1_[0], a1_[1], b1[0], b1[1]};
    };
    rdW(0);
    lgkm0();
    issue_w2(RNG);
    f_steps<0>([&](auto S) {
      constexpr int s = S.value;
      vmwait2<vmF(s)>(act[s % DEP][0], act[s % DEP][1]);
      const u32x4 w0 = n0, w1 = n1;
      if constexpr (s + 1 < NSF) rdW(s + 1);
      if (s < NSF - 2 || s < nst) {
        acc[0][0] = mfma(w0, act[s % DEP][0], acc[0][0]);
        acc[0][1] = mfma(w0, act[s % DEP][1], acc[0][1]);
        acc[1][0] = mfma(w1, act[s % DEP][0], acc[1][0]);
        acc[1][1] = mfma(w1, act[s % DEP][1], acc[1][1]);
      }
      if constexpr (s + 1 + RNG < NSF) { lgkm0(); issue_w2(s + 1 + RNG); }
      if constexpr (s + DEP < NSF) issue_actF(s + DEP);
      sched0();
    });
    STAMP(6);
    write_partial(smem, P2_FE + q * 8192, P2_FL + q * 10240, acc, lane);
  }

  const int c = tid >> 3, dg = tid & 7;
  const int tok = 128 * panel + 64 * rh + c, d0 = 64 * cb + 8 * dg;
  const u32x4 xv = *(const u32x4*)((const char*)p.x + (tok * D_ + d0) * 2);
  const u32x4 dv = *(const u32x4*)((const char*)p.dsk + d0 * 2);
  wg_barrier();
  STAMP(7);
  const f32x2 ab = *(LAS f32x2*)(smem + M_STATS + (64 * rh + c) * 8);
  f32x4 s0 = {}, s1 = {};
  {
    const int cc = c & 31, tt = c >> 5;
    const unsigned o0 = cc * 256 + (((2 * dg) ^ (cc & 15)) << 4), o1 = cc * 256 + (((2 * dg + 1) ^ (cc & 15)) << 4);
#pragma unroll
    for (int k = 0; k < 4; ++k) {
      const unsigned bf = tt ? P2_FL + k * 10240 : P2_FE + k * 8192;
      const unsigned bs = (tt ? P2_SG1 : P2_SG0) + k * 8192;
      s0 += ldf4(smem + bf + o0); s1 += ldf4(smem + bf + o1);
      s0 += ldf4(smem + bs + o0); s1 += ldf4(smem + bs + o1);
    }
  }
  f32x4 o0, o1;
#pragma unroll
  for (int k = 0; k < 2; ++k) {
    const float x0 = bflo(xv[k]), x1 = bfhi(xv[k]), x2 = bflo(xv[k + 2]), x3 = bfhi(xv[k + 2]);
    o0[2 * k] = x0 + fmaf(fmaf(x0, ab[0], ab[1]), bflo(dv[k]), s0[2 * k]);
    o0[2 * k + 1] = x1 + fmaf(fmaf(x1, ab[0], ab[1]), bfhi(dv[k]), s0[2 * k + 1]);
    o1[2 * k] = x2 + fmaf(fmaf(x2, ab[0], ab[1]), bflo(dv[k + 2]), s1[2 * k]);
    o1[2 * k + 1] = x3 + fmaf(fmaf(x3, ab[0], ab[1]), bfhi(dv[k + 2]), s1[2 * k + 1]);
  }
  float* op = p.out + tok * D_ + d0;
  *(f32x4*)op = o0;
  *(f32x4*)(op + 4) = o1;
  if (tid == 0 && panel == 15 && slice == 15) {
    __hip_atomic_store((u64*)(p.ws + WS_EPOCH), tag, __ATOMIC_RELAXED, __HIP_MEMORY_SCOPE_AGENT);
  }
  STAMP(8);
}

extern "C" void kernel_launch(void* const* d_in, const int* in_sizes, int n_in, void* d_out, int out_size, void* d_ws,
                              size_t ws_size, hipStream_t stream) {
  Params p;
  p.x = d_in[0]; p.lre = d_in[1]; p.lim = d_in[2]; p.bre = d_in[3]; p.bim = d_in[4]; p.cre = d_in[5]; p.cim = d_in[6];
  p.ldt = d_in[7]; p.dsk = d_in[8]; p.w1 = d_in[10]; p.w2 = d_in[11];
  p.out = (float*)d_out; p.ws = (char*)d_ws;
  s5ffn_kernel<<<256, 512, 0, stream>>>(p);
}
```

```cpp
#include <hip/hip_runtime.h>
#include <hip/hip_bf16.h>
#include <stdint.h>

#define DI __device__ __forceinline__
#define LAS __attribute__((address_space(3)))
typedef __attribute__((ext_vector_type(8))) __bf16 bf16x8;
typedef __attribute__((ext_vector_type(2))) __bf16 bf16x2;
typedef __attribute__((ext_vector_type(2))) float f32x2;
typedef __attribute__((ext_vector_type(4))) float f32x4;
typedef __attribute__((ext_vector_type(16))) float f32x16;
typedef __attribute__((ext_vector_type(2))) unsigned u32x2;
typedef __attribute__((ext_vector_type(4))) unsigned u32x4;
typedef __attribute__((ext_vector_type(4))) short s16x4;
typedef unsigned long long u64;
typedef __amdgpu_buffer_rsrc_t rsrc_t;

constexpr int L_ = 2048, D_ = 512, F_ = 1376;
constexpr int KSF = F_ / 16;
constexpr int KSS = 64;
constexpr int NSF = 22;
constexpr int RNG = 9;
constexpr int DEP = 8;

constexpr unsigned WS_EPOCH = 0, WS_S = 4096, WS_FLAGF = 0x30000, WS_FLAGS = 0x40000;
constexpr unsigned WS_H1 = 0x100000, WS_H = 0x700000, WS_END = 0xB00000;

constexpr unsigned SLOT = 36864, OFF_B = 16384, OFF_W = 24576;
constexpr unsigned MISC = 4 * SLOT;
constexpr unsigned M_STATS = MISC, M_CS = MISC + 1024, M_EW = MISC + 2560, M_TR = MISC + 4096;
constexpr unsigned M_CSB = MISC + 12288, M_FLG = MISC + 13312, M_CT = MISC + 13376;
constexpr unsigned P2_FE = 0, P2_SG0 = 36 * 1024, P2_SG1 = 72 * 1024, P2_FL = 104 * 1024;
constexpr unsigned LDS_BYTES = 163840;
constexpr int SPIN_CAP = 1 << 18;

DI rsrc_t mkrsrc(const void* p, unsigned bytes) { return __builtin_amdgcn_make_buffer_rsrc((void*)p, (short)0, (int)bytes, 0x00020000); }
DI void dma16(unsigned lds, unsigned voff, rsrc_t srd, unsigned soff) {
  asm volatile("s_mov_b32 m0, %0\n\ts_nop 0\n\tbuffer_load_dwordx4 %1, %2, %3 offen lds" ::"s"(lds), "v"(voff), "s"(srd), "s"(soff) : "m0", "memory");
}
DI void ldu16(unsigned& dst, unsigned voff, rsrc_t srd) {
  asm volatile("buffer_load_ushort %0, %1, %2, 0 offen" : "=v"(dst) : "v"(voff), "s"(srd) : "memory");
}
DI unsigned opaque(unsigned v) { asm volatile("" : "+v"(v)); return v; }
DI void sched0() { __builtin_amdgcn_sched_barrier(0); }
DI void ldact(u32x4& dst, unsigned voff, rsrc_t srd, unsigned soff) {
  asm volatile("buffer_load_dwordx4 %0, %1, %2, %3 offen sc1" : "=v"(dst) : "v"(voff), "s"(srd), "s"(soff) : "memory");
}
DI void touch(unsigned& dst, unsigned voff, rsrc_t srd, unsigned soff) {
  asm volatile("buffer_load_dword %0, %1, %2, %3 offen sc1" : "=v"(dst) : "v"(voff), "s"(srd), "s"(soff) : "memory");
}
template <int N> DI void vmwait() { asm volatile("s_waitcnt vmcnt(%0)" ::"n"(N) : "memory"); }
template <int N> DI void vmwait2(u32x4& a, u32x4& b) { asm volatile("s_waitcnt vmcnt(%2)" : "+v"(a), "+v"(b) : "n"(N) : "memory"); }
DI void lgkm0() { asm volatile("s_waitcnt lgkmcnt(0)" ::: "memory"); }
DI void wg_barrier() { asm volatile("s_waitcnt lgkmcnt(0)\n\ts_barrier" ::: "memory"); }

DI float dot2(unsigned a, unsigned b, float c) {
  return __builtin_amdgcn_fdot2_f32_bf16(__builtin_bit_cast(bf16x2, a), __builtin_bit_cast(bf16x2, b), c, false);
}
DI f32x16 mfma(u32x4 a, u32x4 b, f32x16 c) {
  return __builtin_amdgcn_mfma_f32_32x32x16_bf16(__builtin_bit_cast(bf16x8, a), __builtin_bit_cast(bf16x8, b), c, 0, 0, 0);
}
DI float solo(float v) { asm("" : "+v"(v)); return v; }
typedef __attribute__((ext_vector_type(4))) short i16x4;
DI f32x4 mfma4(unsigned a0, unsigned a1, unsigned b0, unsigned b1, f32x4 c) {
  return __builtin_amdgcn_mfma_f32_4x4x4bf16_1k(__builtin_bit_cast(i16x4, u32x2{a0, a1}), __builtin_bit_cast(i16x4, u32x2{b0, b1}), c, 0, 0, 0);
}
DI float own4(f32x4 v, int lane) {
  const float lo = (lane & 1) ? v[1] : v[0], hi = (lane & 1) ? v[3] : v[2];
  return (lane & 2) ? hi : lo;
}
DI unsigned pack2(float lo, float hi) {
  f32x2 v = {solo(lo), solo(hi)};
  return __builtin_bit_cast(unsigned, __builtin_convertvector(v, bf16x2));
}
DI float bf16r(float v) { return __uint_as_float(pack2(v, 0.f) << 16); }
DI float bflo(unsigned u) { return __uint_as_float(u << 16); }
DI float bfhi(unsigned u) { return __uint_as_float(u & 0xffff0000u); }
DI float lo32(float v) { auto r = __builtin_amdgcn_permlane32_swap(__float_as_uint(v), __float_as_uint(v), false, false); return __uint_as_float(r[0]); }
DI float hi32(float v) { auto r = __builtin_amdgcn_permlane32_swap(__float_as_uint(v), __float_as_uint(v), false, false); return __uint_as_float(r[1]); }
DI float sum32(float v) { auto r = __builtin_amdgcn_permlane32_swap(__float_as_uint(v), __float_as_uint(v), false, false); return solo(__uint_as_float(r[0]) + __uint_as_float(r[1])); }

struct cpx { float r, i; };
DI cpx cmul(cpx a, cpx b) { return {fmaf(a.r, b.r, -solo(a.i * b.i)), fmaf(a.r, b.i, solo(a.i * b.r))}; }
DI cpx cfma(cpx a, cpx b, cpx c) { return {fmaf(a.r, b.r, solo(fmaf(-a.i, b.i, c.r))), fmaf(a.r, b.i, solo(fmaf(a.i, b.r, c.i)))}; }

typedef __attribute__((ext_vector_type(4))) short v4i16_t;
DI u32x2 trread(LAS char* p) { return __builtin_bit_cast(u32x2, __builtin_amdgcn_ds_read_tr16_b64_v4i16((LAS v4i16_t*)p)); }
DI u32x4 ld128(LAS char* p) { return *(LAS u32x4*)p; }
DI f32x4 ldf4(LAS char* p) { return *(LAS f32x4*)p; }

constexpr int vmF(int i) {
  int pos = 0, pa[NSF] = {}, pw[NSF + 1] = {};
  for (int k = 0; k < DEP; ++k) { pos += 2; pa[k] = pos; }
  for (int it = 0; it < i; ++it) {
    if (it + RNG < NSF) { pos += 2; pw[it + RNG] = pos; }
    if (it + DEP < NSF) { pos += 2; pa[it + DEP] = pos; }
  }
  int need = pa[i] > pw[i + 1] ? pa[i] : pw[i + 1];
  return pos - need;
}
constexpr int vmS(int u) {
  int pos = 0, pa[16] = {}, pg[5] = {};
  for (int k = 0; k < DEP; ++k) { pos += 2; pa[k] = pos; }
  for (int it = 0; it < u; ++it) {
    if (it == 2) { pos += 8; pg[2] = pos; }
    if (it == 6) { pos += 8; pg[3] = pos; }
    if (it + DEP < 16) { pos += 2; pa[it + DEP] = pos; }
  }
  int need = pa[u] > pg[(u + 1) >> 2] ? pa[u] : pg[(u + 1) >> 2];
  return pos - need;
}

struct Params {
  const void *x, *lre, *lim, *bre, *bim, *cre, *cim, *ldt, *dsk, *w1, *w2;
  float* out;
  char* ws;
};


template <int I, class T> DI void f_steps(T&& fn) { if constexpr (I < NSF) { fn(std::integral_constant<int, I>{}); f_steps<I + 1>(fn); } }
template <int I, class T> DI void s_steps(T&& fn) { if constexpr (I < 16) { fn(std::integral_constant<int, I>{}); s_steps<I + 1>(fn); } }
template <int I, class T> DI void stages(T&& fn) { if constexpr (I < 8) { fn(std::integral_constant<int, I>{}); stages<I + 1>(fn); } }

DI void write_partial(LAS char* smem, unsigned base0, unsigned base1, const f32x16 (&acc)[2][2], int lane) {
  const int c = lane & 31, h = lane >> 5;
#pragma unroll
  for (int tt = 0; tt < 2; ++tt)
#pragma unroll
    for (int dt = 0; dt < 2; ++dt)
#pragma unroll
      for (int i = 0; i < 4; ++i) {
        f32x4 v = {acc[dt][tt][4 * i], acc[dt][tt][4 * i + 1], acc[dt][tt][4 * i + 2], acc[dt][tt][4 * i + 3]};
        const int slot = 8 * dt + 2 * i + h;
        *(LAS f32x4*)(smem + (tt ? base1 : base0) + c * 256 + ((slot ^ (c & 15)) << 4)) = v;
      }
}

__global__ void __launch_bounds__(512, 2) s5ffn_kernel(Params p) {
  __shared__ __attribute__((aligned(16))) char smem_[LDS_BYTES];
  LAS char* smem = (LAS char*)smem_;
  const unsigned lds0 = (unsigned)(uintptr_t)smem;
  const int tid = threadIdx.x, lane = tid & 63;
  const int w = __builtin_amdgcn_readfirstlane(tid >> 6);
  const int g = w & 3;
  const int xcd = blockIdx.x & 7, r = blockIdx.x >> 3;
  const int panel = 4 * (xcd >> 1) + (r >> 3), slice = 8 * (xcd & 1) + (r & 7);
  const int rh = (r >> 2) & 1, cb = 4 * (xcd & 1) + (r & 3);
  const int l31 = lane & 31, h = lane >> 5;
  const bool isS = w < 4;

  const rsrc_t rX = mkrsrc(p.x, L_ * D_ * 2), rW1 = mkrsrc(p.w1, D_ * F_ * 2);
  const rsrc_t rB = mkrsrc(isS ? p.bre : p.bim, 512 * D_ * 2);
  const rsrc_t rWS = mkrsrc(p.ws, WS_END);

  const unsigned vX0 = (16 * w + (lane >> 3)) * 1024 + (((lane & 7) ^ (lane >> 4)) << 4);
  const unsigned vX1 = (16 * w + 8 + (lane >> 3)) * 1024 + (((lane & 7) ^ (4 + (lane >> 4))) << 4);
  const unsigned vB = (32 * slice + 8 * g + (lane >> 3)) * 1024 + (((lane & 7) ^ ((4 * w + (lane >> 4)) & 7)) << 4);
  auto w1off = [&](int wi) -> unsigned {
    const int c = 64 * wi + lane, row = c / 12, cc = c - 12 * row;
    const int colb = min(192 * slice + 16 * cc, F_ * 2 - 16);
    return row * (F_ * 2) + colb;
  };
  const int wiA = isS ? 2 * g : 8 + g;
  const unsigned vWa = w1off(wiA), vWb = w1off(2 * g + 1);
  const unsigned sXbase = panel * 131072;

  auto issue_part = [&](int st, int part) {
    const unsigned sl = lds0 + (st & 3) * SLOT;
    const unsigned kc = (st + r) & 7;
    if (part == 0) {
      dma16(sl + (2 * w) * 1024, vX0, rX, sXbase + kc * 128);
      if (isS) dma16(sl + OFF_W + (2 * g + 1) * 1024, vWb, rW1, kc * 64 * F_ * 2);
    }
    if (part == 1) dma16(sl + (2 * w + 1) * 1024, vX1, rX, sXbase + kc * 128);
    if (part == 2) dma16(sl + OFF_B + w * 1024, vB, rB, kc * 128);
    if (part == 3) dma16(sl + OFF_W + wiA * 1024, vWa, rW1, kc * 64 * F_ * 2);
  };
  auto issue_stage = [&](int st) {
    const unsigned sl = lds0 + (st & 3) * SLOT;
    const unsigned kc = (st + r) & 7;
    dma16(sl + (2 * w) * 1024, vX0, rX, sXbase + kc * 128);
    dma16(sl + (2 * w + 1) * 1024, vX1, rX, sXbase + kc * 128);
    dma16(sl + OFF_B + w * 1024, vB, rB, kc * 128);
    dma16(sl + OFF_W + wiA * 1024, vWa, rW1, kc * 64 * F_ * 2);
    if (isS) dma16(sl + OFF_W + (2 * g + 1) * 1024, vWb, rW1, kc * 64 * F_ * 2);
  };

  asm volatile("s_nop 4" ::: "memory");
  unsigned plre = 0, plim = 0, pldt = 0;
  const u64 tag = *(const __attribute__((address_space(4))) u64*)(p.ws + WS_EPOCH) + 1;
  if (tid < 4) *(LAS unsigned*)(smem + M_FLG + tid * 4) = 0;
  if (isS) {
    ldu16(plre, slice * 64 + l31 * 2, mkrsrc(p.lre, 1024));
    ldu16(plim, slice * 64 + l31 * 2, mkrsrc(p.lim, 1024));
    ldu16(pldt, slice * 64 + l31 * 2, mkrsrc(p.ldt, 1024));
  }
  unsigned tw0, tw1 = 0;
  {
    const unsigned tg = 4 * panel + g;
    const unsigned own = isS ? WS_H + (tg * KSS + 4 * slice + (lane & 3)) * 1024
                     : 6 * slice < KSF ? WS_H1 + (tg * KSF + min(6 * slice + min(lane, 5), KSF - 1)) * 1024
                                       : WS_H1 + 64 * KSF * 1024;
    touch(tw0, own, rWS, 0);
    if (w == 4) touch(tw1, ((128 * panel + 64 * rh + lane) * D_ + 64 * cb) * 4, mkrsrc(p.out, L_ * D_ * 4), 0);
  }
  issue_stage(0); issue_stage(1); issue_stage(2); issue_stage(3);
  if (isS) asm volatile("s_waitcnt vmcnt(21)" : "+v"(plre), "+v"(plim), "+v"(pldt)::"memory");

  if (isS) {
    const rsrc_t rCre = mkrsrc(p.cre, D_ * 512 * 2), rCim = mkrsrc(p.cim, D_ * 512 * 2);
    const int q = g;
    const unsigned vC0 = (64 * cb + (lane >> 3)) * 1024;
    auto issue_group = [&](int grp, unsigned ldsb) {
      const rsrc_t rc = (grp & 1) ? rCim : rCre;
      const unsigned so = (128 * q + 64 * (grp >> 1)) * 2;
#pragma unroll
      for (int k = 0; k < 8; ++k) {
        const unsigned vo = vC0 + k * 8192 + (((lane & 7) ^ ((4 * k + (lane >> 4)) & 7)) << 4);
        dma16(lds0 + ldsb + k * 1024, vo, rc, so);
      }
    };
    auto issue_gpart = [&](int grp, unsigned ldsb, int part) {
      const rsrc_t rc = (grp & 1) ? rCim : rCre;
      const unsigned so = (128 * q + 64 * (grp >> 1)) * 2;
#pragma unroll
      for (int k = 2 * part; k < 2 * part + 2; ++k) {
        const unsigned vo = vC0 + k * 8192 + (((lane & 7) ^ ((4 * k + (lane >> 4)) & 7)) << 4);
        dma16(lds0 + ldsb + k * 1024, vo, rc, so);
      }
    };
    const int pr = 16 * ((l31 >> 2) & 1) + 4 * (l31 >> 3) + (l31 & 3);
    const int rowx = 32 * g + pr;
    const float lr = bflo(plre), li = bflo(plim), dt = bf16r(expf(bflo(pldt)));
    cpx lam, c2;
    {
      const float er = expf(dt * lr);
      float sn, cs;
      sincosf(dt * li, &sn, &cs);
      lam = {solo(er * cs), solo(er * sn)};
      const float nr = lam.r - 1.f, ni = lam.i, id = 2.f / fmaf(lr, lr, solo(li * li));
      c2 = {solo(fmaf(nr, lr, solo(ni * li)) * id), solo(fmaf(ni, lr, -solo(nr * li)) * id)};
    }
    cpx l16 = cmul(lam, lam); l16 = cmul(l16, l16); l16 = cmul(l16, l16); l16 = cmul(l16, l16);
    const cpx l32 = cmul(l16, l16);
    cpx l128 = cmul(l32, l32); l128 = cmul(l128, l128);
    const unsigned aX0 = rowx * 128 + ((h ^ ((rowx >> 1) & 7)) << 4);
    const unsigned aB0 = OFF_B + l31 * 128 + ((h ^ ((l31 >> 1) & 7)) << 4);
    f32x16 are = {}, aim = {};
    f32x4 sm4 = {}, sq4 = {}, cr4 = {}, ci4 = {};
    const unsigned ONES = 0x3F803F80u;
    u32x4 xf, br, bi;
    unsigned ax, ab_;
    auto rdS = [&](int sub) { xf = ld128(smem + (ax ^ (sub << 5))); br = ld128(smem + (ab_ ^ (sub << 5))); bi = ld128(smem + (ab_ ^ (sub << 5)) + 4096); };
    vmwait<15>();
    asm volatile("" ::"v"(tw0));
    wg_barrier();
    ax = opaque(aX0); ab_ = opaque(aB0);
    rdS(0);
    stages<0>([&](auto ST) {
      constexpr int st = ST.value;
#pragma unroll
      for (int sub = 0; sub < 4; ++sub) {
        const u32x4 cx = xf, cr = br, ci = bi;
        if (sub < 3) {
          rdS(sub + 1);
          if constexpr (st >= 1 && st <= 4) issue_part(st + 3, sub + 1);
          if constexpr (st == 6) issue_gpart(0, P2_SG0 + q * 8192, sub + 1);
          if constexpr (st == 7) issue_gpart(1, P2_SG1 + q * 8192, sub + 1);
        } else if (st < 7) {
          lgkm0();
          vmwait<st <= 4 ? 10 : st == 5 ? 5 : 8>();
          wg_barrier();

          if constexpr (st <= 3) issue_part(st + 4, 0);
          if constexpr (st == 5) issue_gpart(0, P2_SG0 + q * 8192, 0);
          if constexpr (st == 6) issue_gpart(1, P2_SG1 + q * 8192, 0);
          ax = opaque(aX0 + ((st + 1) & 3) * SLOT); ab_ = opaque(aB0 + ((st + 1) & 3) * SLOT);
          rdS(0);
        }
        are = mfma(cx, cr, are);
        aim = mfma(cx, ci, aim);
        sm4 = mfma4(cx[0], cx[1], ONES, ONES, sm4); sq4 = mfma4(cx[0], cx[1], cx[0], cx[1], sq4);
        sm4 = mfma4(cx[2], cx[3], ONES, ONES, sm4); sq4 = mfma4(cx[2], cx[3], cx[2], cx[3], sq4);
        if (sub == g) {
          cr4 = mfma4(cr[0], cr[1], ONES, ONES, cr4); ci4 = mfma4(ci[0], ci[1], ONES, ONES, ci4);
          cr4 = mfma4(cr[2], cr[3], ONES, ONES, cr4); ci4 = mfma4(ci[2], ci[3], ONES, ONES, ci4);
        }
        sched0();
      }
    });
    {
      const float mean = solo(sum32(own4(sm4, lane)) * (1.f / 512.f));
      const float var = fmaxf(fmaf(sum32(own4(sq4, lane)), 1.f / 512.f, -solo(mean * mean)), 0.f);
      const float m = bf16r(mean);
      const float den = bf16r(__fsqrt_rn(bf16r(bf16r(var) + 1e-5f)));
      const float a = 1.f / den;
      if (h == 0) *(LAS f32x2*)(smem + M_STATS + rowx * 8) = f32x2{solo(a), solo(-m * a)};
    }
    {
      const float pr_ = sum32(own4(cr4, lane)), pi_ = sum32(own4(ci4, lane));
      *(LAS float*)(smem + M_CSB + g * 256 + lane * 4) = h ? pi_ : pr_;
    }
    wg_barrier();
    float csr = 0.f, csi = 0.f;
#pragma unroll
    for (int k = 0; k < 4; ++k) {
      csr = solo(csr + *(LAS float*)(smem + M_CSB + k * 256 + l31 * 4));
      csi = solo(csi + *(LAS float*)(smem + M_CSB + k * 256 + 128 + l31 * 4));
    }
    float ur[16], ui[16];
#pragma unroll
    for (int i = 0; i < 16; i += 2) {
      const f32x4 ab = ldf4(smem + M_STATS + (32 * g + 16 * h + i) * 8);
      ur[i] = fmaf(ab[0], are[i], ab[1] * csr); ui[i] = fmaf(ab[0], aim[i], ab[1] * csi);
      ur[i + 1] = fmaf(ab[2], are[i + 1], ab[3] * csr); ui[i + 1] = fmaf(ab[2], aim[i + 1], ab[3] * csi);
    }
    cpx e = {ur[0], ui[0]};
#pragma unroll
    for (int i = 1; i < 16; ++i) e = cfma(lam, e, cpx{ur[i], ui[i]});
    const cpx e0 = {lo32(e.r), lo32(e.i)}, e1 = {hi32(e.r), hi32(e.i)};
    const cpx ew = cfma(l16, e0, e1);
    if (h == 0) *(LAS f32x2*)(smem + M_EW + (g * 32 + l31) * 8) = f32x2{solo(ew.r), solo(ew.i)};
    if (lane == 0) *(volatile LAS unsigned*)(smem + M_FLG + g * 4) = 1u;
    for (int spin = 0; spin < SPIN_CAP; ++spin) {
      const unsigned v = *(volatile LAS unsigned*)(smem + M_FLG + (lane & 3) * 4);
      if (__all(v == 1u)) break;
    }
    asm volatile("" ::: "memory");
    cpx pre = {0.f, 0.f};
    {
      cpx s = {0.f, 0.f};
#pragma unroll
      for (int k = 0; k < 4; ++k) {
        if (k == g) pre = s;
        const f32x2 v = *(LAS f32x2*)(smem + M_EW + (k * 32 + l31) * 8);
        s = cfma(l32, s, cpx{v[0], v[1]});
      }
      if (g == 3 && h == 0) {
        u32x4 gr = {__float_as_uint(s.r), (unsigned)tag, __float_as_uint(s.i), (unsigned)(tag >> 32)};
        __builtin_amdgcn_raw_buffer_store_b128(gr, rWS, WS_S + ((panel * 16 + slice) * 32 + l31) * 16, 0, 16);
      }
    }
    cpx hin = {0.f, 0.f};
    if (panel > 0) {
      u32x4 gv[8];
      __builtin_amdgcn_s_sleep(40);
      for (int spin = 0; spin < SPIN_CAP; ++spin) {
        const u32x4 t = __builtin_amdgcn_raw_buffer_load_b128(rWS, WS_S + ((panel - 1) * 16 + slice) * 32 * 16, 0, 16);
        if (__all(t[1] == (unsigned)tag && t[3] == (unsigned)(tag >> 32))) break;
        __builtin_amdgcn_s_sleep(14);
      }
      for (int spin = 0; spin < SPIN_CAP; ++spin) {
        bool ok = true;
#pragma unroll
        for (int k = 0; k < 8; ++k) {
          const int pp = min(8 * h + k, panel - 1);
          gv[k] = __builtin_amdgcn_raw_buffer_load_b128(rWS, WS_S + ((pp * 16 + slice) * 32 + l31) * 16, 0, 16);
        }
#pragma unroll
        for (int k = 0; k < 8; ++k) ok = ok && gv[k][1] == (unsigned)tag && gv[k][3] == (unsigned)(tag >> 32);
        if (__all(ok)) break;
        __builtin_amdgcn_s_sleep(14);
      }
      cpx acc = {0.f, 0.f}, pw = {1.f, 0.f};
#pragma unroll
      for (int k = 0; k < 8; ++k) {
        const bool v = 8 * h + k < panel;
        const cpx na = cfma(l128, acc, cpx{__uint_as_float(gv[k][0]), __uint_as_float(gv[k][2])});
        const cpx np = cmul(l128, pw);
        acc.r = v ? na.r : acc.r; acc.i = v ? na.i : acc.i;
        pw.r = v ? np.r : pw.r; pw.i = v ? np.i : pw.i;
      }
      hin = cfma(cpx{hi32(pw.r), hi32(pw.i)}, cpx{lo32(acc.r), lo32(acc.i)}, cpx{hi32(acc.r), hi32(acc.i)});
      hin = {solo(hin.r), solo(hin.i)};
    }
    {
      cpx lg = {1.f, 0.f};
#pragma unroll
      for (int k = 0; k < 3; ++k) { const cpx t = cmul(lg, l32); if (k < g) lg = t; }
      cpx c = cfma(lg, hin, pre);
      const cpx c1 = cfma(l16, c, e0);
      if (h) c = c1;
      unsigned ore[8], oim[8];
#pragma unroll
      for (int i = 0; i < 16; i += 2) {
        c = cfma(lam, c, cpx{ur[i], ui[i]});
        const float r0 = fmaf(c2.r, c.r, -solo(c2.i * c.i)), i0 = fmaf(-c2.r, c.i, -solo(c2.i * c.r));
        c = cfma(lam, c, cpx{ur[i + 1], ui[i + 1]});
        const float r1 = fmaf(c2.r, c.r, -solo(c2.i * c.i)), i1 = fmaf(-c2.r, c.i, -solo(c2.i * c.r));
        ore[i >> 1] = pack2(r0, r1); oim[i >> 1] = pack2(i0, i1);
      }
      LAS char* tr = smem + M_TR + g * 2048;
      const int g16 = lane >> 4, q4 = (lane & 15) >> 2, p4 = lane & 3;
      const unsigned trb = (8 * (g16 >> 1) + q4) * 64 + (16 * (g16 & 1) + 4 * p4) * 2;
      const unsigned tg = 4 * panel + g;
#pragma unroll
      for (int part = 0; part < 2; ++part) {
        const unsigned* o = part ? oim : ore;
        *(LAS u32x4*)(tr + l31 * 64 + 32 * h) = u32x4{o[0], o[1], o[2], o[3]};
        *(LAS u32x4*)(tr + l31 * 64 + 32 * h + 16) = u32x4{o[4], o[5], o[6], o[7]};
        lgkm0();
#pragma unroll
        for (int s = 0; s < 2; ++s) {
          const u32x2 a = trread(tr + trb + s * 1024), b = trread(tr + trb + s * 1024 + 256);
          __builtin_amdgcn_raw_buffer_store_b128(u32x4{a[0], a[1], b[0], b[1]}, rWS,
                                                 WS_H + (tg * KSS + 4 * slice + 2 * part + s) * 1024 + lane * 16, 0, 16);
        }
        lgkm0();
      }
      vmwait<0>();
      if (lane == 0)
        __hip_atomic_store((u64*)(p.ws + WS_FLAGS + ((panel * 4 + g) * 16 + slice) * 64), tag, __ATOMIC_RELAXED, __HIP_MEMORY_SCOPE_AGENT);
    }
    {
      __builtin_amdgcn_s_sleep(32);
      const u64* fl = (const u64*)(p.ws + WS_FLAGS + ((panel * 4 + 2 * rh + ((lane >> 2) & 1)) * 16 + 4 * q + (lane & 3)) * 64);
      for (int spin = 0; spin < SPIN_CAP; ++spin) {
        const u64 v = __hip_atomic_load(fl, __ATOMIC_RELAXED, __HIP_MEMORY_SCOPE_AGENT);
        if (__all(v == tag)) break;
        __builtin_amdgcn_s_sleep(14);
      }
    }
    f32x16 acc[2][2] = {};
    u32x4 act[DEP][2];
    const unsigned vL = lane * 16;
    const unsigned hb = WS_H + (4 * panel + 2 * rh) * KSS * 1024;
    auto ksS = [&](int u) { return 4 * (4 * q + 2 * (u >> 3) + ((u & 3) >> 1)) + 2 * ((u >> 2) & 1) + (u & 1); };
    auto issue_actS = [&](int u) {
      ldact(act[u % DEP][0], vL, rWS, hb + ksS(u) * 1024);
      ldact(act[u % DEP][1], vL, rWS, hb + (KSS + ksS(u)) * 1024);
    };
#pragma unroll
    for (int u = 0; u < DEP; ++u) issue_actS(u);
    const unsigned aC0 = l31 * 128 + ((h ^ ((l31 >> 1) & 7)) << 4) + q * 8192;
    const unsigned aCa = opaque(aC0 + P2_SG0), aCb = opaque(aC0 + P2_SG1);
    u32x4 c0, c1;
    auto rdC = [&](int u) { const unsigned a = (((u >> 2) & 1) ? aCb : aCa) ^ ((u & 3) << 5); c0 = ld128(smem + a); c1 = ld128(smem + a + 4096); };
    rdC(0);
    s_steps<0>([&](auto U) {
      constexpr int u = U.value;
      vmwait2<vmS(u)>(act[u % DEP][0], act[u % DEP][1]);
      const u32x4 w0 = c0, w1 = c1;
      if constexpr (u < 15) rdC(u + 1);
      acc[0][0] = mfma(w0, act[u % DEP][0], acc[0][0]);
      acc[0][1] = mfma(w0, act[u % DEP][1], acc[0][1]);
      acc[1][0] = mfma(w1, act[u % DEP][0], acc[1][0]);
      acc[1][1] = mfma(w1, act[u % DEP][1], acc[1][1]);
      if constexpr (u == 2 || u == 6) { lgkm0(); issue_group(u == 2 ? 2 : 3, (u == 2 ? P2_SG0 : P2_SG1) + q * 8192); }
      if constexpr (u + DEP < 16) issue_actS(u + DEP);
      sched0();
    });
    write_partial(smem, P2_SG0 + q * 8192, P2_SG1 + q * 8192, acc, lane);
  } else {
    const rsrc_t rW2 = mkrsrc(p.w2, F_ * D_ * 2);
    const int q = g;
    const int nst = min(NSF, KSF - NSF * q);
    const unsigned vW2 = (lane >> 3) * 1024 + 128 * cb + (((lane & 7) ^ (((lane >> 4) & 1) << 2)) << 4);
    auto ring = [&](int i) -> unsigned { return i < 4 ? P2_FE + q * 8192 + i * 2048 : P2_FL + q * 10240 + (i - 4) * 2048; };
    auto issue_w2 = [&](int s) {
      const int ks = min(NSF * q + s, KSF - 1);
      const unsigned lb = lds0 + ring(s % RNG);
      dma16(lb, vW2, rW2, ks * 16 * 1024);
      dma16(lb + 1024, vW2, rW2, ks * 16 * 1024 + 8192);
    };
    const int rowx = 32 * g + l31;
    const int g16 = lane >> 4, q4 = (lane & 15) >> 2, p4 = lane & 3;
    const unsigned aX0 = rowx * 128 + ((h ^ ((rowx >> 1) & 7)) << 4);
    const unsigned aW0 = OFF_W + (8 * (g16 >> 1) + q4) * 192 + (16 * (g16 & 1) + 4 * p4) * 2;
    f32x16 a1[3] = {};
    f32x4 cs[3] = {};
    const unsigned ONES = 0x3F803F80u;
    u32x4 xf, wf[3];
    unsigned ax, aw;
    auto rdF = [&](int sub) {
      xf = ld128(smem + (ax ^ (sub << 5)));
#pragma unroll
      for (int t = 0; t < 3; ++t) {
        const u32x2 a = trread(smem + aw + sub * 16 * 192 + t * 64), b = trread(smem + aw + (sub * 16 + 4) * 192 + t * 64);
        wf[t] = u32x4{a[0], a[1], b[0], b[1]};
      }
    };
    unsigned pf0, pf1;
    {
      const int i = 43 * g + min(lane, 42);
      touch(pf0, (43 * r + (i >> 2)) * 1024 + 512 * (xcd & 1) + (i & 3) * 128, rW2, 0);
      const rsrc_t rC = mkrsrc((g & 1) ? p.cim : p.cre, D_ * 512 * 2);
      touch(pf1, (256 * (xcd & 1) + 8 * r) * 1024 + lane * 128, rC, 0);
    }
    vmwait<14>();
    asm volatile("" ::"v"(tw0), "v"(tw1));
    wg_barrier();
    ax = opaque(aX0); aw = opaque(aW0);
    rdF(0);
    stages<0>([&](auto ST) {
      constexpr int st = ST.value;
#pragma unroll
      for (int sub = 0; sub < 4; ++sub) {
        const u32x4 cx = xf, c0 = wf[0], c1 = wf[1], c2 = wf[2];
        if (sub < 3) {
          rdF(sub + 1);
          if constexpr (st >= 1 && st <= 4) issue_part(st + 3, sub + 1);
          if constexpr (st == 5) issue_w2(sub + 1);
        } else if (st < 7) {
          lgkm0();
          vmwait<st <= 2 ? 10 : st <= 4 ? 8 : st == 5 ? 12 : 8>();
          wg_barrier();

          if constexpr (st <= 3) issue_part(st + 4, 0);
          if constexpr (st == 4) issue_w2(0);
          ax = opaque(aX0 + ((st + 1) & 3) * SLOT); aw = opaque(aW0 + ((st + 1) & 3) * SLOT);
          rdF(0);
        }
        a1[0] = mfma(c0, cx, a1[0]);
        a1[1] = mfma(c1, cx, a1[1]);
        a1[2] = mfma(c2, cx, a1[2]);
        if (sub == g) {
          cs[0] = mfma4(c0[0], c0[1], ONES, ONES, cs[0]); cs[1] = mfma4(c1[0], c1[1], ONES, ONES, cs[1]); cs[2] = mfma4(c2[0], c2[1], ONES, ONES, cs[2]);
          cs[0] = mfma4(c0[2], c0[3], ONES, ONES, cs[0]); cs[1] = mfma4(c1[2], c1[3], ONES, ONES, cs[1]); cs[2] = mfma4(c2[2], c2[3], ONES, ONES, cs[2]);
        }
        sched0();
      }
    });
    asm volatile("" ::"v"(pf0), "v"(pf1));
#pragma unroll
    for (int t = 0; t < 3; ++t) {
      const float v = sum32(own4(cs[t], lane));
      if (h == 0) *(LAS float*)(smem + M_CS + g * 384 + (32 * t + l31) * 4) = v;
    }
    wg_barrier();
    {
      const f32x2 ab = *(LAS f32x2*)(smem + M_STATS + rowx * 8);
      const unsigned tg = 4 * panel + g;
      {
        const unsigned c0_ = M_CS + lane * 4, c1_ = M_CS + (64 + l31) * 4;
        float t0 = 0.f, t1 = 0.f;
#pragma unroll
        for (int k = 0; k < 4; ++k) { t0 = solo(t0 + *(LAS float*)(smem + c0_ + k * 384)); t1 = solo(t1 + *(LAS float*)(smem + c1_ + k * 384)); }
        *(LAS float*)(smem + M_CT + g * 384 + lane * 4) = t0;
        *(LAS float*)(smem + M_CT + g * 384 + (64 + l31) * 4) = t1;
      }
      f32x4 call[12];
#pragma unroll
      for (int j = 0; j < 12; ++j) call[j] = ldf4(smem + M_CT + g * 384 + (8 * j + 4 * h) * 4);
      sched0();
#pragma unroll
      for (int t = 0; t < 3; ++t) {
        unsigned o[8];
#pragma unroll
        for (int i = 0; i < 4; ++i) {
          const f32x4 c4 = call[4 * t + i];
          float v[4];
#pragma unroll
          for (int k = 0; k < 4; ++k) v[k] = fmaxf(fmaf(ab[0], a1[t][4 * i + k], ab[1] * c4[k]), 0.f);
          o[2 * i] = pack2(v[0], v[1]); o[2 * i + 1] = pack2(v[2], v[3]);
        }
#pragma unroll
        for (int s = 0; s < 2; ++s) {
          const int ks = 6 * slice + 2 * t + s;
          if (ks < KSF)
            __builtin_amdgcn_raw_buffer_store_b128(u32x4{o[4 * s], o[4 * s + 1], o[4 * s + 2], o[4 * s + 3]}, rWS,
                                                   WS_H1 + (tg * KSF + ks) * 1024 + lane * 16, 0, 16);
        }
      }
    }
#pragma unroll
    for (int s = 4; s < RNG; ++s) issue_w2(s);
    vmwait<2 * (RNG - 4)>();
    if (lane == 0)
      __hip_atomic_store((u64*)(p.ws + WS_FLAGF + ((panel * 4 + g) * 16 + slice) * 64), tag, __ATOMIC_RELAXED, __HIP_MEMORY_SCOPE_AGENT);
    {
      const int slo = (NSF * q) / 6, shi = (NSF * q + nst - 1) / 6;
      const int sl = min(slo + (lane & 7), shi);
      __builtin_amdgcn_s_sleep(24);
      const u64* fl = (const u64*)(p.ws + WS_FLAGF + ((panel * 4 + 2 * rh + ((lane >> 3) & 1)) * 16 + sl) * 64);
      for (int spin = 0; spin < SPIN_CAP; ++spin) {
        const u64 v = __hip_atomic_load(fl, __ATOMIC_RELAXED, __HIP_MEMORY_SCOPE_AGENT);
        if (__all(v == tag)) break;
        __builtin_amdgcn_s_sleep(14);
      }
    }
    vmwait<0>();
    f32x16 acc[2][2] = {};
    u32x4 act[DEP][2];
    const unsigned vL = lane * 16;
    const unsigned hb = WS_H1 + (4 * panel + 2 * rh) * KSF * 1024;
    auto issue_actF = [&](int s) {
      const int ks = min(NSF * q + s, KSF - 1);
      ldact(act[s % DEP][0], vL, rWS, hb + ks * 1024);
      ldact(act[s % DEP][1], vL, rWS, hb + (KSF + ks) * 1024);
    };
#pragma unroll
    for (int s = 0; s < DEP; ++s) issue_actF(s);
    const unsigned aT = (4 * (g16 >> 1) + q4) * 128;
    const unsigned colb = (16 * (g16 & 1) + 4 * p4) * 2, swp = ((q4 >> 1) & 1) << 6;
    const unsigned aT0 = aT + (colb ^ swp), aT1 = aT + ((64 + colb) ^ swp);
    u32x4 n0, n1;
    auto rdW = [&](int s) {
      const unsigned sb = ring(s % RNG);
      const unsigned t0 = opaque(aT0 + sb), t1 = opaque(aT1 + sb);
      const u32x2 a0 = trread(smem + t0), b0 = trread(smem + t0 + 1024), a1_ = trread(smem + t1), b1 = trread(smem + t1 + 1024);
      n0 = u32x4{a0[0], a0[1], b0[0], b0[1]}; n1 = u32x4{a1_[0], a1_[1], b1[0], b1[1]};
    };
    rdW(0);
    f_steps<0>([&](auto S) {
      constexpr int s = S.value;
      vmwait2<vmF(s)>(act[s % DEP][0], act[s % DEP][1]);
      const u32x4 w0 = n0, w1 = n1;
      if constexpr (s + 1 < NSF) rdW(s + 1);
      if (s < NSF - 2 || s < nst) {
        acc[0][0] = mfma(w0, act[s % DEP][0], acc[0][0]);
        acc[0][1] = mfma(w0, act[s % DEP][1], acc[0][1]);
        acc[1][0] = mfma(w1, act[s % DEP][0], acc[1][0]);
        acc[1][1] = mfma(w1, act[s % DEP][1], acc[1][1]);
      }
      sched0();
      if constexpr (s + RNG < NSF) issue_w2(s + RNG);
      if constexpr (s + DEP < NSF) issue_actF(s + DEP);
      sched0();
    });
    write_partial(smem, P2_FE + q * 8192, P2_FL + q * 10240, acc, lane);
  }

  const int c = tid >> 3, dg = tid & 7;
  const int tok = 128 * panel + 64 * rh + c, d0 = 64 * cb + 8 * dg;
  const u32x4 xv = *(const u32x4*)((const char*)p.x + (tok * D_ + d0) * 2);
  const u32x4 dv = *(const u32x4*)((const char*)p.dsk + d0 * 2);
  wg_barrier();
  const f32x2 ab = *(LAS f32x2*)(smem + M_STATS + (64 * rh + c) * 8);
  f32x4 s0 = {}, s1 = {};
  {
    const int cc = c & 31, tt = c >> 5;
    const unsigned o0 = cc * 256 + (((2 * dg) ^ (cc & 15)) << 4), o1 = cc * 256 + (((2 * dg + 1) ^ (cc & 15)) << 4);
#pragma unroll
    for (int k = 0; k < 4; ++k) {
      const unsigned bf = tt ? P2_FL + k * 10240 : P2_FE + k * 8192;
      const unsigned bs = (tt ? P2_SG1 : P2_SG0) + k * 8192;
      s0 += ldf4(smem + bf + o0); s1 += ldf4(smem + bf + o1);
      s0 += ldf4(smem + bs + o0); s1 += ldf4(smem + bs + o1);
    }
  }
  f32x4 o0, o1;
#pragma unroll
  for (int k = 0; k < 2; ++k) {
    const float x0 = bflo(xv[k]), x1 = bfhi(xv[k]), x2 = bflo(xv[k + 2]), x3 = bfhi(xv[k + 2]);
    o0[2 * k] = x0 + fmaf(fmaf(x0, ab[0], ab[1]), bflo(dv[k]), s0[2 * k]);
    o0[2 * k + 1] = x1 + fmaf(fmaf(x1, ab[0], ab[1]), bfhi(dv[k]), s0[2 * k + 1]);
    o1[2 * k] = x2 + fmaf(fmaf(x2, ab[0], ab[1]), bflo(dv[k + 2]), s1[2 * k]);
    o1[2 * k + 1] = x3 + fmaf(fmaf(x3, ab[0], ab[1]), bfhi(dv[k + 2]), s1[2 * k + 1]);
  }
  float* op = p.out + tok * D_ + d0;
  *(f32x4*)op = o0;
  *(f32x4*)(op + 4) = o1;
  if (tid == 0 && panel == 15 && slice == 15) {
    __hip_atomic_store((u64*)(p.ws + WS_EPOCH), tag, __ATOMIC_RELAXED, __HIP_MEMORY_SCOPE_AGENT);
  }
}

extern "C" void kernel_launch(void* const* d_in, const int* in_sizes, int n_in, void* d_out, int out_size, void* d_ws,
                              size_t ws_size, hipStream_t stream) {
  Params p;
  p.x = d_in[0]; p.lre = d_in[1]; p.lim = d_in[2]; p.bre = d_in[3]; p.bim = d_in[4]; p.cre = d_in[5]; p.cim = d_in[6];
  p.ldt = d_in[7]; p.dsk = d_in[8]; p.w1 = d_in[10]; p.w2 = d_in[11];
  p.out = (float*)d_out; p.ws = (char*)d_ws;
  s5ffn_kernel<<<256, 512, 0, stream>>>(p);
}
```
